```python
import torch

def _softmax(x, axis=-1):
    return torch.softmax(x, dim=axis)
def _gelu(x):
    return torch.nn.functional.gelu(x, approximate="tanh")
def _silu(x):
    return torch.nn.functional.silu(x)
def _sigmoid(x):
    return torch.sigmoid(x)
def _layernorm(x, axis=-1, eps=1e-5):
    m = x.mean(dim=axis, keepdim=True)
    v = x.var(dim=axis, keepdim=True, unbiased=False)
    return (x - m) / torch.sqrt(v + eps)
def _scatter_add(tgt, idx, src, axis=0):
    return tgt.clone().index_add_(axis, idx, src)
def _segment_sum(src, seg, n):
    out = torch.zeros((n,) + src.shape[1:], dtype=src.dtype)
    return out.index_add_(0, seg, src)
def _pad(x, pw):
    flat = tuple(v for pair in reversed(pw) for v in pair)
    return torch.nn.functional.pad(x, flat)

kernel_name = 'gated_mix__ssm_conv_h3__grouped_kv_mqa__prompt'

def setup_inputs():

    _w = (lambda a: (lambda t: t.to(torch.bfloat16) if t.is_floating_point() else t)(torch.as_tensor(a)))
    def _A_setup():
        B, S, D = 1, 2048, 1024
        ks = 4
        return {
            "q": _w((lambda *s: torch.randn(*s, dtype=torch.bfloat16) * 0.05)(B, S, D)),
            "k": _w((lambda *s: torch.randn(*s, dtype=torch.bfloat16) * 0.4)(B, S, D)),
            "v": _w((lambda *s: torch.randn(*s, dtype=torch.bfloat16) * 0.4)(B, S, D)),
            "shift_k": _w((lambda *s: torch.randn(*s, dtype=torch.bfloat16) * 0.4)(D, ks) * 0.5),
            "ssm_k": _w((lambda *s: torch.randn(*s, dtype=torch.bfloat16) * 0.8)(D, S) * (1.0 / S ** 0.5)),
            "ssm_D": _w((lambda *s: torch.randn(*s, dtype=torch.bfloat16) * 0.4)(D)),
        }
    _A_in = _A_setup()

    def _B_setup():
        B, S, D, H = 1, 2048, 1024, 32
        Dh = D // H
        return {
            "x": _w((lambda *s: torch.randn(*s, dtype=torch.bfloat16) * 0.05)(B, S, D)),
            "wq": _w((lambda *s: torch.randn(*s, dtype=torch.bfloat16) * 0.8)(D, H * Dh)),
            "wk": _w((lambda *s: torch.randn(*s, dtype=torch.bfloat16) * 0.8)(D, Dh)),
            "wv": _w((lambda *s: torch.randn(*s, dtype=torch.bfloat16) * 0.05)(D, Dh)),
            "wo": _w((lambda *s: torch.randn(*s, dtype=torch.bfloat16) * 0.05)(H * Dh, D)),
        }
    _B_in = _B_setup()

    return {
        "A_q": _A_in["q"],
        "A_k": _A_in["k"],
        "A_v": _A_in["v"],
        "A_shift_k": _A_in["shift_k"],
        "A_ssm_k": _A_in["ssm_k"],
        "A_ssm_D": _A_in["ssm_D"],
        "B_x": _B_in["x"],
        "B_wq": _B_in["wq"],
        "B_wk": _B_in["wk"],
        "B_wv": _B_in["wv"],
        "B_wo": _B_in["wo"],
        "gate_w": _w((lambda *s: torch.randn(*s, dtype=torch.bfloat16) * 0.05)(1024) * 16.0),
    }

def reference(A_q, A_k, A_v, A_shift_k, A_ssm_k, A_ssm_D, B_x, B_wq, B_wk, B_wv, B_wo, gate_w):
    def _A_ref(q, k, v, shift_k, ssm_k, ssm_D):
        B, S, D = q.shape
        ks = shift_k.shape[-1]
        k_pad = _pad(k, ((0, 0), (ks - 1, 0), (0, 0)))
        k_shift = torch.zeros_like(k)
        for t in range(ks):
            k_shift = k_shift + k_pad[:, t:t + S, :] * shift_k[:, ks - 1 - t][None, None, :]
        kv = k_shift * v
        L = 2 * S
        kv_pad = _pad(kv, ((0, 0), (0, S), (0, 0)))
        kf_pad = _pad(ssm_k, ((0, 0), (0, S)))
        kv_f = (lambda x, axes=(-2,-1): torch.fft.rfft2(x.float(), dim=axes))(kv_pad[:, None, :, :], axes=(1, 2))
        kf_f = (lambda x, axes=(-2,-1): torch.fft.rfft2(x.float(), dim=axes))(kf_pad[:, None, :], axes=(1, 2))
        kf_f = (lambda x, *d: x.permute(*d))(kf_f, 1, 2, 0)
        y_f = kv_f * kf_f[None, :, :, :]
        y = (lambda x, s, axes=(-2,-1): torch.fft.irfft2(x, s=s, dim=axes).to(torch.bfloat16))(y_f, (1, L), axes=(1, 2))[:, 0, :S, :]
        y = y + kv * ssm_D[None, None, :]
        return q * y

    def _B_ref(x, wq, wk, wv, wo):
        B, S, D = x.shape
        H = 32
        Dh = D // H
        q = (lambda x, *d: x.permute(*d))((x @ wq).reshape(B, S, H, Dh), 0, 2, 1, 3)
        k = x @ wk
        v = x @ wv
        scores = torch.einsum("bhqd,bkd->bhqk", q, k) * (Dh ** -0.5)
        p = _softmax(scores, axis=-1)
        o = torch.einsum("bhqk,bkd->bhqd", p, v)
        return (lambda x, *d: x.permute(*d))(o, 0, 2, 1, 3).reshape(B, S, H * Dh) @ wo

    x = A_q
    g = _sigmoid((x * gate_w).sum(-1, keepdims=True))
    a = _A_ref(x, A_k, A_v, A_shift_k, A_ssm_k, A_ssm_D)
    b = _B_ref(x, B_wq, B_wk, B_wv, B_wo)
    return g * a + (1 - g) * b
```

```cpp
#include <hip/hip_runtime.h>
#include <hip/hip_bf16.h>
#include <stdint.h>

#define DI __device__ __forceinline__
#define LAS __attribute__((address_space(3)))
#define GAS __attribute__((address_space(1)))
typedef __bf16 bf16x2 __attribute__((ext_vector_type(2)));
typedef __bf16 bf16x8 __attribute__((ext_vector_type(8)));
typedef __bf16 bf16x16 __attribute__((ext_vector_type(16)));
typedef float f32x2 __attribute__((ext_vector_type(2)));
typedef float f32x4 __attribute__((ext_vector_type(4)));
typedef float f32x8 __attribute__((ext_vector_type(8)));
typedef float f32x16 __attribute__((ext_vector_type(16)));
typedef unsigned u32x2 __attribute__((ext_vector_type(2)));
typedef unsigned u32x4 __attribute__((ext_vector_type(4)));
typedef short s16x4 __attribute__((ext_vector_type(4)));
typedef __amdgpu_buffer_rsrc_t rsrc_t;
typedef unsigned long long u64;
typedef const LAS char* lptr;

constexpr unsigned WS_EPOCH = 0, WS_ERR = 256;
constexpr unsigned WS_FKV = 4096, WS_FU = 6144;
constexpr unsigned WS_FO = 32768, WS_FY = 49152;
constexpr unsigned WS_G = 16384;
constexpr unsigned WS_KV = 65536;
constexpr unsigned WS_U = 1u << 20;
constexpr unsigned WS_O = 5u << 20;
constexpr unsigned WS_Y = 9u << 20;
constexpr unsigned WS_END = 13u << 20;
constexpr unsigned L_XS = 0;
constexpr unsigned L_RING = 65536, SLOT = 20480;
constexpr unsigned L_CR = L_RING + 3 * SLOT;
constexpr unsigned L_GATE = L_CR + 32768, L_GP = L_CR + 34816;
constexpr unsigned L_KIMG = L_CR, L_VIMG = L_CR + 17408, L_SHIFT = L_CR + 33792;
constexpr unsigned L_U4 = 6 * 8192;
constexpr unsigned L_H4 = L_CR, H_PITCH = 4128, H_ODD = 4 * H_PITCH + 64;
constexpr unsigned L_OX = L_RING + SLOT, L_LX = L_CR + 33792;
constexpr unsigned L_YT = L_CR + 4096;
constexpr unsigned L_SY = L_CR + 35840;
constexpr unsigned LDS_BYTES = 163840;
constexpr unsigned SPIN_CAP = 1u << 18;
constexpr int AUX_SC1 = 16;
constexpr unsigned OOR = 0x80000000u;

#define SBAR() __builtin_amdgcn_sched_barrier(0)
template <class T> DI void PIN(T& x) { asm volatile("" : "+v"(x)); }
#define PRIO(N) asm volatile("s_setprio %0" :: "i"(N))
#define VMWAIT(N) asm volatile("s_waitcnt vmcnt(%0)" :: "i"(N) : "memory")
#define WAITBAR(N) asm volatile("s_waitcnt vmcnt(%0) lgkmcnt(0)\n\ts_barrier" :: "i"(N) : "memory")
#define BARRIER() asm volatile("s_waitcnt lgkmcnt(0)\n\ts_barrier" ::: "memory")
#define MFMA32(a, b, c) __builtin_amdgcn_mfma_f32_32x32x16_bf16(a, b, c, 0, 0, 0)
#define SMF32(a, b, c) __builtin_amdgcn_smfmac_f32_32x32x32_bf16(a, b, c, 0xDDDD, 0, 0)
#define SMF16(a, b, c) __builtin_amdgcn_smfmac_f32_16x16x64_bf16(a, b, c, 0xDDDD, 0, 0)

template <int V> struct Ic { static constexpr int v = V; };
template <int I, int N, class F> DI void static_for(F&& f) { if constexpr (I < N) { f(Ic<I>{}); static_for<I + 1, N>(f); } }
#define SFOR(var, N) static_for<0, N>([&](auto var##_c) __attribute__((always_inline)) { constexpr int var = decltype(var##_c)::v;
#define SEND });
#define INL __attribute__((always_inline))

DI rsrc_t mk_rsrc(const void* p, unsigned bytes) { return __builtin_amdgcn_make_buffer_rsrc((void*)p, (short)0, (int)bytes, 0x00020000); }
#define DMA_FIRST(MOD) "s_mov_b32 m0, %1\n\ts_mov_b32 %0, %4\n\ts_nop 2\n\tbuffer_load_dwordx4 %2, %3, %0 offen " MOD "lds\n\t"
#define DMA_NEXT(MOD) "s_add_u32 m0, m0, %5\n\ts_add_u32 %0, %0, %6\n\tbuffer_load_dwordx4 %2, %3, %0 offen " MOD "lds\n\t"
#define DMA_OPS : "=&s"(t) : "s"(lds), "v"(voff), "s"(r), "s"(soff), "i"(LS), "i"(GS) : "memory", "scc"
#define DMA_BODY(MOD) \
  unsigned t; lds = __builtin_amdgcn_readfirstlane(lds); soff = __builtin_amdgcn_readfirstlane(soff); \
  if constexpr (N == 1) asm volatile(DMA_FIRST(MOD) DMA_OPS); \
  else if constexpr (N == 2) asm volatile(DMA_FIRST(MOD) DMA_NEXT(MOD) DMA_OPS); \
  else if constexpr (N == 4) asm volatile(DMA_FIRST(MOD) DMA_NEXT(MOD) DMA_NEXT(MOD) DMA_NEXT(MOD) DMA_OPS); \
  else { static_assert(N == 7, "piece count"); \
    asm volatile(DMA_FIRST(MOD) DMA_NEXT(MOD) DMA_NEXT(MOD) DMA_NEXT(MOD) DMA_NEXT(MOD) DMA_NEXT(MOD) DMA_NEXT(MOD) DMA_OPS); }
template <int N = 1, int LS = 1024, int GS = 1024> DI void dma16(unsigned lds, unsigned voff, rsrc_t r, unsigned soff) { DMA_BODY("") }
template <int N = 1, int LS = 1024, int GS = 1024> DI void dma16_dev(unsigned lds, unsigned voff, rsrc_t r, unsigned soff) { DMA_BODY("sc1 ") }
template <int LI, int GI> DI void dma16i(unsigned sl, unsigned voff, rsrc_t r, unsigned sg) {
  unsigned t;
  asm volatile("s_add_u32 m0, %1, %5\n\ts_add_u32 %0, %4, %6\n\tbuffer_load_dwordx4 %2, %3, %0 offen lds" : "=&s"(t) : "s"(sl), "v"(voff), "s"(r), "s"(sg), "i"(LI), "i"(GI) : "memory", "scc");
}
template <int LI, int GI> DI void dma16i_dev(unsigned sl, unsigned voff, rsrc_t r, unsigned sg) {
  unsigned t;
  asm volatile("s_add_u32 m0, %1, %5\n\ts_add_u32 %0, %4, %6\n\tbuffer_load_dwordx4 %2, %3, %0 offen sc1 lds" : "=&s"(t) : "s"(sl), "v"(voff), "s"(r), "s"(sg), "i"(LI), "i"(GI) : "memory", "scc");
}
DI void dma4(unsigned lds, unsigned voff, rsrc_t r, unsigned soff) {
  asm volatile("s_mov_b32 m0, %0\n\ts_nop 3\n\tbuffer_load_dword %1, %2, %3 offen lds" :: "s"(lds), "v"(voff), "s"(r), "s"(soff) : "memory");
}
DI void store16_dev(u32x4 d, rsrc_t r, unsigned voff, unsigned soff) {
  __builtin_amdgcn_raw_buffer_store_b128(d, r, voff, soff, AUX_SC1);
  asm volatile("s_nop 1" :: "v"(d));
}
DI bf16x8 lds_b128(lptr p) { return *(const LAS bf16x8*)p; }
DI s16x4 lds_tr(lptr p) { return __builtin_amdgcn_ds_read_tr16_b64_v4i16((LAS s16x4*)p); }
DI bf16x8 lds_tr2(lptr p0, lptr p1) { const s16x4 a = lds_tr(p0), b = lds_tr(p1); return __builtin_bit_cast(bf16x8, __builtin_shufflevector(a, b, 0, 1, 2, 3, 4, 5, 6, 7)); }
DI unsigned pk2(float lo, float hi) { const f32x2 v = {lo, hi}; return __builtin_bit_cast(unsigned, __builtin_convertvector(v, bf16x2)); }
DI float bf_lo(unsigned u) { return __builtin_bit_cast(float, u << 16); }
DI float bf_hi(unsigned u) { return __builtin_bit_cast(float, u & 0xffff0000u); }
DI bf16x8 pack8(const float* v) { const u32x4 u = {pk2(v[0], v[1]), pk2(v[2], v[3]), pk2(v[4], v[5]), pk2(v[6], v[7])}; return __builtin_bit_cast(bf16x8, u); }
DI bf16x16 raw_lo(const f32x16& p) { return __builtin_bit_cast(bf16x16, __builtin_shufflevector(p, p, 0, 1, 2, 3, 4, 5, 6, 7)); }
DI bf16x16 raw_hi(const f32x16& p) { return __builtin_bit_cast(bf16x16, __builtin_shufflevector(p, p, 8, 9, 10, 11, 12, 13, 14, 15)); }
DI float from_lane(float v, int lane) { return __builtin_bit_cast(float, __builtin_amdgcn_ds_bpermute(lane * 4, __builtin_bit_cast(int, v))); }
DI bool wave_any(bool c) { return __builtin_amdgcn_ballot_w64(c) != 0; }

DI void put_flag(rsrc_t rws, unsigned voff, unsigned off, u64 tag) {
  const u32x2 t = {(unsigned)tag, (unsigned)(tag >> 32)};
  __builtin_amdgcn_raw_buffer_store_b64(t, rws, voff, off, AUX_SC1);
}
template <int M> DI void wait_flags(char* ws, unsigned off, u64 tag, int l) {
  GAS u64* f = (GAS u64*)(ws + off) + (l & M);
#pragma unroll 1
  for (unsigned spins = 0;; ++spins) {
    if (!wave_any(__hip_atomic_load(f, __ATOMIC_RELAXED, __HIP_MEMORY_SCOPE_AGENT) != tag)) break;
    if (spins > SPIN_CAP) { __hip_atomic_store((GAS unsigned*)(ws + WS_ERR), 1u, __ATOMIC_RELAXED, __HIP_MEMORY_SCOPE_AGENT); break; }
    __builtin_amdgcn_s_sleep(1);
  }
  __builtin_amdgcn_fence(__ATOMIC_ACQUIRE, "wavefront");
}
template <int M> DI void flags_ask(u32x2& f, rsrc_t rws, unsigned off, int l) {
  asm volatile("s_nop 4\n\tbuffer_load_dwordx2 %0, %1, %2, %3 offen sc1" : "=v"(f) : "v"((l & M) * 8), "s"(rws), "s"(off) : "memory");
}
template <int M> DI void flags_done(u32x2& f, char* ws, unsigned off, u64 tag, int l) {
  asm volatile("" : "+v"(f) :: "memory");
  if (__builtin_expect(wave_any((f[0] != (unsigned)tag) | (f[1] != (unsigned)(tag >> 32))), 0)) wait_flags<M>(ws, off, tag, l);
}
template <int OFF> DI void load8(u32x2& d, unsigned voff, rsrc_t r, unsigned soff) {
  asm volatile("buffer_load_dwordx2 %0, %1, %2, %3 offen offset:%4" : "=v"(d) : "v"(voff), "s"(r), "s"(soff), "i"(OFF) : "memory");
}
DI void touch(unsigned& d, unsigned voff, rsrc_t r, unsigned soff) {
  asm volatile("buffer_load_dword %0, %1, %2, %3 offen" : "=v"(d) : "v"(voff), "s"(r), "s"(soff) : "memory");
}
DI void load4_dev(unsigned& d, unsigned voff, rsrc_t r, unsigned soff) {
  asm volatile("buffer_load_dword %0, %1, %2, %3 offen sc1" : "=v"(d) : "v"(voff), "s"(r), "s"(soff) : "memory");
}
template <int N> DI void vmwait_for(u32x4& d) { asm volatile("s_waitcnt vmcnt(%1)" : "+v"(d) : "i"(N) : "memory"); }
template <int DW> DI void read_h8(u32x2& a, u32x2& b, unsigned addr) {
  asm volatile("ds_read2_b32 %0, %2 offset0:%3 offset1:%4\n\tds_read2_b32 %1, %2 offset0:%5 offset1:%6"
               : "=&v"(a), "=&v"(b) : "v"(addr), "i"(DW), "i"(DW + 1), "i"(DW + 2), "i"(DW + 3) : "memory");
}
template <int OFF> DI void read_tr2(s16x4& a, s16x4& b, unsigned addr) {
  asm volatile("ds_read_b64_tr_b16 %0, %2 offset:%3\n\tds_read_b64_tr_b16 %1, %2 offset:%4" : "=&v"(a), "=&v"(b) : "v"(addr), "i"(OFF), "i"(OFF + 1024) : "memory");
}
template <int OFF> DI void read_tr2y(s16x4& a, s16x4& b, unsigned addr) {
  asm volatile("ds_read_b64_tr_b16 %0, %2 offset:%3\n\tds_read_b64_tr_b16 %1, %2 offset:%4" : "=&v"(a), "=&v"(b) : "v"(addr), "i"(OFF), "i"(OFF + 512) : "memory");
}
template <int N> DI void lgkmwait_for(s16x4& a, s16x4& b, s16x4& c, s16x4& d) { asm volatile("s_waitcnt lgkmcnt(%4)" : "+v"(a), "+v"(b), "+v"(c), "+v"(d) : "i"(N) : "memory"); }
template <int OFF> DI void read_u8(bf16x8& d, unsigned addr) { asm volatile("ds_read_b128 %0, %1 offset:%2" : "=v"(d) : "v"(addr), "i"(OFF) : "memory"); }
template <bool FIRST> DI void conv_mfma(f32x4& acc, const u32x2& a0, const u32x2& a1, const bf16x8& b) {
  const u32x4 a = __builtin_shufflevector(a0, a1, 0, 1, 2, 3);
  if constexpr (FIRST) asm volatile("v_mfma_f32_16x16x32_bf16 %0, %1, %2, 0" : "=&v"(acc) : "v"(a), "v"(b));
  else asm volatile("v_mfma_f32_16x16x32_bf16 %0, %1, %2, %0" : "+v"(acc) : "v"(a), "v"(b));
}
DI void lds_signal(LAS char* p) { asm volatile("" ::: "memory"); *(volatile LAS unsigned*)p = 1u; }
DI void lds_await(LAS char* p, char* ws) {
#pragma unroll 1
  for (unsigned spins = 0;; ++spins) {
    const u32x4 v = *(volatile LAS u32x4*)p;
    if (__builtin_amdgcn_readfirstlane(v[0] & v[1] & v[2] & v[3])) break;
    if (spins > 64 * SPIN_CAP) { __hip_atomic_store((GAS unsigned*)(ws + WS_ERR), 1u, __ATOMIC_RELAXED, __HIP_MEMORY_SCOPE_AGENT); break; }
    __builtin_amdgcn_s_sleep(1);
  }
  asm volatile("" ::: "memory");
}
DI void step_blk(int& kb) {
  asm volatile("s_add_u32 %0, %0, 1\n\ts_cmp_eq_u32 %0, 6\n\ts_cselect_b32 %0, 8, %0\n\ts_cmp_eq_u32 %0, 15\n\ts_cselect_b32 %0, 0, %0" : "+s"(kb) :: "scc");
}
DI void fresh(int& a) { asm volatile("" : "+s"(a)); }
template <int OFF> DI void load16_dev(u32x4& d, unsigned voff, rsrc_t r, unsigned soff) {
  asm volatile("buffer_load_dwordx4 %0, %1, %2, %3 offen offset:%4 sc1" : "=v"(d) : "v"(voff), "s"(r), "s"(soff), "i"(OFF) : "memory");
}

#if defined(__HIP_DEVICE_COMPILE__)
#define NO_PK __attribute__((target("no-packed-fp32-ops")))
#else
#define NO_PK
#endif
__global__ NO_PK void __launch_bounds__(512, 2)
fused_kernel(const __hip_bfloat16* __restrict__ px, const __hip_bfloat16* __restrict__ pk, const __hip_bfloat16* __restrict__ pv,
             const __hip_bfloat16* __restrict__ pshift, const __hip_bfloat16* __restrict__ ph, const __hip_bfloat16* __restrict__ pD,
             const __hip_bfloat16* __restrict__ pwq, const __hip_bfloat16* __restrict__ pwk, const __hip_bfloat16* __restrict__ pwv,
             const __hip_bfloat16* __restrict__ pwo, const __hip_bfloat16* __restrict__ pgate, __hip_bfloat16* pout, char* ws) {
  __shared__ __attribute__((aligned(1024))) char lds_raw[LDS_BYTES];
  LAS char* const lds = (LAS char*)lds_raw;
  const unsigned L0 = (unsigned)(uintptr_t)lds;
  const int l = __builtin_amdgcn_workitem_id_x() & 63;
  int w = __builtin_amdgcn_readfirstlane(__builtin_amdgcn_workitem_id_x() >> 6);
  const int r32 = l & 31, h = l >> 5, i16 = l & 15, q4 = i16 >> 2, p4 = l & 3;
  const int bx = __builtin_amdgcn_workgroup_id_x(), xcd = bx & 7, jj = bx >> 3, tbl = jj >> 3;
  int tb = (xcd >> 1) * 4 + tbl, hp = (xcd & 1) * 8 + (jj & 7);
  int tb2 = 2 * xcd + (jj >> 4), cp = jj & 15;
  const int tgs = hp & 3, kvt = hp >> 2;
  int T0 = tb * 128, tg = w & 3, kh = w >> 2;
  unsigned warm_code;
  {
    u64 pc; unsigned len;
    asm volatile("s_getpc_b64 %0\nfused_code_begin:\n\ts_mov_b32 %1, fused_code_end_a - fused_code_begin\n\ts_max_u32 %1, %1, fused_code_end_b - fused_code_begin"
                 : "=s"(pc), "=s"(len) :: "scc");
    asm volatile("s_nop 4\n\tbuffer_load_dword %0, %1, %2, 0 offen" : "=v"(warm_code) : "v"(l < 2 ? ((jj * 8 + w) * 2 + l) * 128 : OOR), "s"(mk_rsrc((const void*)pc, len + 1024)) : "memory");
  }
  SBAR();

  const rsrc_t rx = mk_rsrc(px, 4u << 20), rwq = mk_rsrc(pwq, 2u << 20), rwo = mk_rsrc(pwo, 2u << 20);
  const rsrc_t rws = mk_rsrc(ws, WS_END);

  u32x2 epoch;
  const unsigned vXa = (l >> 3) * 2048 + (((l & 7) ^ (l >> 4)) << 4);
  const unsigned vXb = (8 + (l >> 3)) * 2048 + (((l & 7) ^ (4 + (l >> 4))) << 4);
  const unsigned vW = (l >> 3) * 2048 + (((l & 7) ^ (((l >> 4) & 1) << 2)) << 4);
  const int rot = (hp + tb) & 15;
  u64 tag;
  auto read_tag = [&]() INL {
    asm volatile("" : "+v"(epoch), "+v"(warm_code) :: "memory");
    tag = (((u64)(unsigned)__builtin_amdgcn_readfirstlane(epoch[1]) << 32) | (unsigned)__builtin_amdgcn_readfirstlane(epoch[0])) + 1;
  };
  const rsrc_t rwx = mk_rsrc(kvt < 2 ? pwk : pwv, 65536);
  unsigned dw, h01;
  bf16x8 qf[2][2];
  unsigned xoff[4];
#pragma unroll
  for (int i = 0; i < 4; ++i) xoff[i] = r32 * 128 + (((2 * i + h) ^ ((r32 >> 1) & 7)) << 4);
  const unsigned vXw = (tg & 1) ? vXb : vXa;
  const unsigned sxr = (T0 + 16 * (tg >> 1)) * 2048;
  const unsigned sx0 = sxr + (tgs == 0) * 65536, sx1 = sxr + (1 + (tgs <= 1)) * 65536, sx2 = sxr + (2 + (tgs <= 2)) * 65536;
  auto issue_stage = [&](int s) INL {
    const unsigned kb = (s + rot) & 15, slot = L0 + L_RING + ((s + 2) % 3) * SLOT;
    dma16<2, 1024, 16384>(slot + 12288 + tg * 2048, vW, rwq, (kb * 64 + 16 * tg) * 2048 + hp * 128);
    dma16(slot + tg * 1024, vXw, rx, sx0 + kb * 128);
    dma16(slot + 4096 + tg * 1024, vXw, rx, sx1 + kb * 128);
    dma16(slot + 8192 + tg * 1024, vXw, rx, sx2 + kb * 128);
    if (s < 3) dma16(L0 + L_XS + kb * 4096 + tg * 1024, vXw, rx, sxr + tgs * 65536 + kb * 128);
  };
  const unsigned vWx = (l >> 1) * 64 + (l & 1) * 16;
  auto issue_xs = [&](int r) INL { const unsigned kb = (r + rot) & 15; dma16(L0 + L_XS + kb * 4096 + tg * 1024, vXw, rx, sxr + tgs * 65536 + kb * 128); };
  auto issue_wx = [&](int r) INL { const unsigned kb = (r + rot) & 15; dma16<2, 1024, 2048>(L0 + L_CR + kb * 2048, vWx, rwx, kb * 4096 + (kvt & 1) * 32); };
  if (kh) issue_stage(0);
  asm volatile("s_nop 4\n\tbuffer_load_dwordx2 %0, off, %1, 0 sc1" : "=v"(epoch) : "s"(rws) : "memory");
  asm volatile("s_load_dword %0, %2, %4\n\ts_load_dword %1, %3, %5" : "=&s"(dw), "=&s"(h01)
               : "s"(pD), "s"(ph), "s"(((64 * hp + 4 * tb + tg) >> 1) * 4), "s"((64 * hp + 4 * tb + tg) * 4096));
  fresh(kh);
  if (kh) {
    issue_stage(1); issue_stage(2);
    const rsrc_t rg = mk_rsrc(pgate, 2048);
    dma4(L0 + L_GATE + tg * 512, l * 4, rg, tg * 512);
    dma4(L0 + L_GATE + tg * 512 + 256, l * 4, rg, tg * 512 + 256);
#pragma unroll
    for (int r = 3; r < 8; ++r) issue_xs(r);
    issue_wx(tg); issue_wx(4 + tg);
  }
  asm volatile("s_waitcnt lgkmcnt(0)" : "+s"(dw), "+s"(h01));
  const int g4 = l >> 4;
  unsigned hb[4], ubase, trow, vl16, vta, vtb, vhalo, pv0, pk0, pk1, psh, pzero, vu, vxm, vflag, kvo, h01d, vfw; int uo7; u32x4 ones_u;
  auto lane_setup = [&](int part) INL {
    int l_ = l; PIN(l_);
    const int i16_ = l_ & 15, g4_ = l_ >> 4, tq = g4_ + 4 * kh, q4_ = i16_ >> 2;
    if (part == 0) {
      const unsigned hst = i16_ + 1 + 8 * g4_;
#pragma unroll
      for (int i = 0; i < 4; ++i) { hb[i] = L0 + L_H4 + tg * H_PITCH + (hst & 1) * H_ODD + (hst >> 1) * 4 + i * 1024; PIN(hb[i]); }
      uo7 = (2032 - 1792 - 16 * i16_ + 8 * g4_) * 2; ubase = L0 + L_U4 + tg * 4096 + uo7;
      h01d = (pk2(bf_lo(h01) + ((tg & 1) ? bf_hi(dw) : bf_lo(dw)), 0.f) & 0xffffu) | (h01 & 0xffff0000u);
      PIN(uo7); PIN(ubase); PIN(h01d);
    } else if (part == 1) {
      trow = tb2 * 128 + 32 * tg + (l_ & 31); vl16 = l_ * 16; vxm = kh ? trow * 2048 + 8 * (l_ >> 5) : OOR; kvo = L_RING + kh * 8192 + vl16;
      vflag = (w == 4 && l_ == 0) ? 0u : OOR;
      const unsigned one2 = 0x3f803f80u, oa = (i16_ & 7) == 0 ? one2 : 0u, ob = (i16_ & 7) == 4 ? one2 : 0u;
      ones_u = u32x4{oa, oa, ob, ob};
      PIN(trow); PIN(vl16); PIN(vxm); PIN(kvo); PIN(vflag); PIN(ones_u);
    } else if (part == 2) {
      vta = ((l_ >> 3) * 2048 + (((((l_ & 7) >> 1) ^ (g4_ & 1)) * 2 + (l_ & 1)) << 4)) ^ ((w & 1) * 64); vtb = vta ^ 64;
      unsigned hv = (l_ >> 5) * 2048 + (((((l_ & 31) >> 3) ^ (tg & 1)) * 32) | ((l_ & 7) * 4)); PIN(hv);
      vhalo = kh ? l_ * 4 : hv | (tb ? 0u : OOR);
      vfw = l_ == 0 ? 0u : OOR;
      PIN(vta); PIN(vtb); PIN(vhalo); PIN(vfw);
    } else {
      const unsigned psw = (unsigned)(((tg ^ ((q4_ >> 1) | ((tq & 1) << 1))) * 32) + 8 * (l_ & 3));
      pv0 = L_VIMG + (16 * tq + q4_) * 128 + psw; pk0 = pv0 + (L_KIMG + 512 - L_VIMG); pk1 = pk0 ^ 64;
      psh = L_SHIFT + (16 * tg + i16_) * 8; pzero = L_H4 + tg * H_PITCH + ((l_ >> 3) & 1) * H_ODD + (l_ & 7) * 4;
      vu = ((64 * hp + 16 * tg + i16_) * 2048 + 2032 - T0 - 16 * ((g4_ & 1) + 4 * kh) + 8 * (g4_ >> 1)) * 2;
      PIN(pv0); PIN(pk0); PIN(pk1); PIN(psh); PIN(pzero); PIN(vu);
    }
  };
  f32x16 o0, o1; f32x4 l0, l1;
  auto clear_o = [&]() INL { o0 = f32x16{}; o1 = f32x16{}; l0 = f32x4{}; l1 = f32x4{}; PIN(o0); PIN(o1); PIN(l0); PIN(l1); };
  unsigned sW1 = L0 + w * 1024, sW2 = L0 + w * 2048, sWo = w * 16384 + cp * 128, sTg = (T0 + 16 * w) * 2048 + hp * 128;
  unsigned sHl = L0 + L_H4 + tg * H_PITCH + 32 + kh * 2048, sUl = L0 + L_U4 + tg * 4096 + kh * 2048, sCh = (64 * hp + 4 * tb + tg) * 4096 + kh * 2048;
  asm volatile("" : "+s"(sW1), "+s"(sW2), "+s"(sWo), "+s"(sTg), "+s"(sHl), "+s"(sUl), "+s"(sCh));
  if (kh == 0) {
    f32x16 acc0 = {}, acc1 = {};
    unsigned a0 = L_RING + 12288 + (8 * h + q4) * 128 + (q4 >> 1) * 64 + 32 * ((l >> 4) & 1) + 8 * p4, a1 = a0 ^ 64;
    unsigned wxo = L_CR + (8 * h + q4) * 32 + 8 * p4;
    PIN(a0); PIN(a1); PIN(wxo);
    bf16x8 fx[2][4], fw[2][8];
    int kbx = rot - 1; unsigned xb = 0;
    const unsigned xring = L_RING + (tg - (tg > tgs)) * 4096;
    auto read_x = [&](int s, int i) INL {
      if (i == 0) { fresh(kbx); kbx = (kbx + 1) & 15; xb = tg == tgs ? L_XS + kbx * 4096 : xring + ((s + 2) % 3) * SLOT; }
      fx[s & 1][i] = lds_b128(lds + (xb | xoff[i]));
    };
    auto read_w = [&](int s, int i) INL {
      const lptr wb = lds + ((i & 1) ? a1 : a0) + ((s + 2) % 3) * SLOT + (i >> 1) * 2048;
      fw[s & 1][i] = lds_tr2(wb, wb + 512);
    };
    auto read_gap = [&](int s, int g) INL {
      if (g == 0) { read_x(s, 0); read_w(s, 0); }
      if (g == 1) { read_w(s, 1); read_x(s, 1); }
      if (g == 2) { read_w(s, 2); read_w(s, 3); }
      if (g == 3) { read_x(s, 2); read_w(s, 4); }
      if (g == 4) { read_w(s, 5); read_x(s, 3); }
      if (g == 5) { read_w(s, 6); read_w(s, 7); }
    };
    f32x16 kvacc = {}; float rd[8];
    bf16x8 xf[2][2], wf[2][2];
    int kbk = 0;
    auto kv_read = [&](int c, int j) INL {
      if (!(j & 1)) { kbk = rot; fresh(kbk); kbk = (kbk + 8 * c + 2 * tg + (j >> 1)) & 15; }
      const lptr wp = lds + (wxo + kbk * 2048);
#pragma unroll
      for (int i = 0; i < 2; ++i) {
        const int st = 2 * (j & 1) + i;
        xf[j & 1][i] = lds_b128(lds + ((L_XS + kbk * 4096) | xoff[st]));
        wf[j & 1][i] = lds_tr2(wp + st * 512, wp + st * 512 + 128);
      }
    };
    auto kv_step = [&](int c, int j) INL {
#pragma unroll
      for (int i = 0; i < 2; ++i) { kvacc = MFMA32(wf[j & 1][i], xf[j & 1][i], kvacc); PIN(kvacc); SBAR(); }
      if (j < 2) kv_read(c, j + 2);
      if (c == 1 && j == 3) {
        *(LAS f32x4*)(lds + L_CR + kbk * 2048 + l * 16) = __builtin_shufflevector(kvacc, kvacc, 0, 1, 2, 3);
        *(LAS f32x4*)(lds + L_CR + kbk * 2048 + 1024 + l * 16) = __builtin_shufflevector(kvacc, kvacc, 4, 5, 6, 7);
      }
      SBAR();
    };
    const int fe = 16 * tg + (l >> 2), fd = l & 3, f32_ = fe & 31, fh = fe >> 5, f16_ = fe & 15, fs = (fe >> 4) & 1;
    const int fk = f32_ ^ (((f32_ >> 2 ^ f32_ >> 3) & 1) * 12);
    const int ft = ((2 * fd) & 3) + 8 * (2 * fs + (fd >> 1)) + 4 * fh;
    unsigned ra0 = L_CR + (kvt < 2 ? (fk + 32 * fh) * 16 + (fd >> 1) * 1024 + (fd & 1) * 8 : (ft + 32 * ((f16_ >> 2) & 1)) * 16 + (f16_ >> 3) * 1024 + (f16_ & 3) * 4);
    unsigned ra1 = ra0 + (kvt < 2 ? 4 : 16);
    unsigned vo = (kvt < 2 ? kvt * 1024 + fe * 16 : (2 + fs) * 1024 + (16 * (kvt - 2) + f16_ + 32 * fh) * 16) + fd * 4;
    const float ks = kvt < 2 ? 0.17677669529663687f * 1.4426950408889634f : 1.f;
    unsigned vf0 = (w == 0 && l == 0) ? 0u : OOR;
    PIN(ra0); PIN(ra1); PIN(vo); PIN(vf0);
    lane_setup(0); lane_setup(1); lane_setup(2); lane_setup(3);
    auto put_q = [&](int hd, const f32x16& a) INL {
#pragma unroll
      for (int i = 0; i < 2; ++i) {
        const u32x4 t = {pk2(a[8 * i], a[8 * i + 1]), pk2(a[8 * i + 2], a[8 * i + 3]), pk2(a[8 * i + 4], a[8 * i + 5]), pk2(a[8 * i + 6], a[8 * i + 7])};
        qf[1 - hd][i] = __builtin_bit_cast(bf16x8, t);
        *(LAS bf16x8*)(lds + L_XS + tg * 4096 + (2 * hd + i) * 1024 + l * 16) = qf[1 - hd][i];
      }
    };
    WAITBAR(0);
    read_tag();
#pragma unroll
    for (int g = 0; g < 6; ++g) read_gap(0, g);
    SFOR(s, 16)
      if constexpr (s == 5) WAITBAR(0); else if constexpr (s > 0) BARRIER();
      if constexpr (s == 5) put_flag(rws, vf0, WS_FKV + (tb * 16 + hp) * 8, tag);
#pragma unroll
      for (int i = 0; i < 8; ++i) {
        const int m = s < 15 ? i : 2 * (i & 3) + (i >> 2);
        if (m & 1) { acc1 = MFMA32(fw[s & 1][m], fx[s & 1][m >> 1], acc1); PIN(acc1); } else { acc0 = MFMA32(fw[s & 1][m], fx[s & 1][m >> 1], acc0); PIN(acc0); }
        SBAR();
        if ((s == 1 || s == 2) && i < 2) kv_read(s - 1, i);
        if (s < 15) read_gap(s + 1, i);
        if (s == 15 && i == 0) clear_o();
        if (s == 15 && i == 6) put_q(0, acc0);
        SBAR();
        if ((s == 1 || s == 2) && (i == 3 || i == 5)) kv_step(s - 1, (i - 3) >> 1);
        if ((s == 1 || s == 2) && i == 7) { kv_step(s - 1, 2); kv_step(s - 1, 3); }
        if (s == 3 && i == 0) {
          int r_ = rot; fresh(r_);
#pragma unroll
          for (int m = 0; m < 4; ++m) {
            const unsigned kb = (r_ + 9 + 2 * m) & 15;
            rd[2 * m] = *(const LAS float*)(lds + (kb * 2048 + ra0)); rd[2 * m + 1] = *(const LAS float*)(lds + (kb * 2048 + ra1));
          }
        }
        if (s == 3 && i == 5)
          __builtin_amdgcn_raw_buffer_store_b32(pk2(ks * ((rd[0] + rd[2]) + (rd[4] + rd[6])), ks * ((rd[1] + rd[3]) + (rd[5] + rd[7]))), rws, vo, WS_KV + (tb * 4 + tgs) * 4096, AUX_SC1);
        SBAR();
      }
    SEND
    put_q(1, acc1);
    BARRIER();
  } else {
    u32x2 fk; f32x16 gacc = {};
    WAITBAR(17);
    read_tag();
#pragma unroll
    for (int r = 8; r < 16; ++r) issue_xs(r);
    issue_wx(8 + tg); issue_wx(12 + tg);
    clear_o(); lane_setup(0); lane_setup(1); lane_setup(2); lane_setup(3);
    SFOR(sn, 15)
      constexpr int s = sn + 1;
      if constexpr (s == 13) { VMWAIT(5); flags_done<63>(fk, ws, WS_FKV + tg * 512, tag, l); }
      if constexpr (s == 1) WAITBAR(12); else if constexpr (s <= 13) WAITBAR(5); else if constexpr (s == 14) WAITBAR(4); else BARRIER();
      bf16x8 xg[4], gf[4];
      if constexpr (s >= 1 && s <= 4) {
        const unsigned kb = (rot + 8 * ((s - 1) >> 1) + 2 * tg + ((s - 1) & 1)) & 15;
#pragma unroll
        for (int i = 0; i < 4; ++i) { xg[i] = lds_b128(lds + ((L_XS + kb * 4096) | xoff[i])); gf[i] = lds_b128(lds + L_GATE + kb * 128 + i * 32 + 16 * h); }
      }
      SBAR();
      if constexpr (s == 12) flags_ask<63>(fk, rws, WS_FKV + tg * 512, l);
      if constexpr (s == 1) issue_stage(3);
      if constexpr (s <= 12) issue_stage(s + 3);
      SBAR();
      if constexpr (s >= 1 && s <= 4) {
#pragma unroll
        for (int i = 0; i < 4; ++i) gacc = MFMA32(gf[i], xg[i], gacc);
        PIN(gacc);
      }
      if constexpr (s == 5) *(LAS float*)(lds + L_GP + tg * 256 + l * 4) = gacc[0];
      if constexpr (s >= 13) dma16_dev<4>(L0 + L_RING + (s - 13) * SLOT + tg * 4096, vl16, rws, WS_KV + ((tb + s - 13) & 15) * 16384 + tg * 4096);
    SEND
    WAITBAR(8);
#pragma unroll
    for (int i = 0; i < 4; ++i) qf[i >> 1][i & 1] = lds_b128(lds + L_XS + tg * 4096 + i * 1024 + l * 16);
  }

  asm volatile("" : "+s"(w), "+s"(tb), "+s"(hp), "+s"(tb2), "+s"(cp));
  T0 = tb * 128; tg = w & 3; kh = w >> 2;
  u32x2 hl[2][8], hh[2][8]; bf16x8 ub[8];
#define READ_H(be, rr) read_h8<((be) & 1) * 128 + 16 * (rr)>(hl[(rr) & 1][be], hh[(rr) & 1][be], hb[(be) >> 1])
#define READ_U(de, rr) do { if constexpr ((de) != 0) read_u8<(7 - (de)) * 512 + (rr) * 64>(ub[de], ubase); \
    else read_u8<0>(ub[0], uo7 + 7 * 512 + (rr) * 64 <= 4080 ? ubase + 7 * 512 + (rr) * 64 : L0 + L_H4); } while (0)
  u32x2 xv[8]; unsigned warm_wo;
  const bf16x8 ones = __builtin_bit_cast(bf16x8, ones_u);
  const f32x16 zero16 = {};
  {
    f32x16 sa, sb;
    bf16x8 k0, k1, v0, v1;
    const lptr kvp = lds + kvo;
    k0 = lds_b128(kvp); k1 = lds_b128(kvp + 1024); v0 = lds_b128(kvp + 2048);
    sa = MFMA32(k0, qf[0][0], zero16); sa = MFMA32(k1, qf[0][1], sa);
    s16x4 tk[5], tv[4]; u32x2 shw, fl;
    const rsrc_t rk = mk_rsrc(pk, 4u << 20), rv = mk_rsrc(pv, 4u << 20), rh = mk_rsrc(ph, 4u << 20);
    int kvb = tb + 2; unsigned kvg = 0;
    auto side = [&](auto s_c, auto i_c) INL {
      constexpr int s = decltype(s_c)::v, i = decltype(i_c)::v;
      if constexpr (s == -1 && i < 3) dma16i<L_VIMG + (i - 1) * 1024, (i - 1) * 16384>(sW2, vta, rv, sTg);
      if constexpr (s == -1 && i == 3) dma16i<L_KIMG + 1024, 0>(sW2, vta, rk, sTg);
      if constexpr (s == 0 && i == 2) dma16i<L_KIMG + 2048, 16384>(sW2, vtb, rk, sTg);
      if constexpr (s == 0 && i == 3) {
        int w_ = w; fresh(w_);
        const int tg_ = w_ & 3, kh_ = w_ >> 2;
        dma4(L0 + (kh_ ? L_SHIFT + (tg_ & 1) * 256 : L_KIMG + tg_ * 256), vhalo, mk_rsrc(kh_ ? pshift : pk, kh_ ? 8192 : 4u << 20),
             kh_ ? hp * 512 + (tg_ & 1) * 256 : tb ? (tb * 128 - 8 + 2 * tg_) * 2048 + hp * 128 : 0);
      }
      if constexpr (i == 0 && s == 6) put_flag(rws, vflag, WS_FU + (hp * 16 + tb) * 8, tag);
      if constexpr (i == 0 && s == 10) flags_ask<15>(fl, rws, WS_FU + hp * 128, l);
      if constexpr (i == 0 && s >= 0 && s < 13) { fresh(kvb); kvb = (kvb + 1) & 15; kvg = kvb * 16384 + w * 2048; }
      if constexpr (i < 2 && s >= 0 && s < 13) dma16i_dev<L_RING + (s % 3) * SLOT + i * 1024, WS_KV + i * 1024>(sW2, vl16, rws, kvg);
      if constexpr (i == 3 && s >= 4 && s <= 9) {
        constexpr int kb = s - 4;
        dma16i<L_XS + kb * 8192, kb * 131072>(sW1, vW, rwo, sWo);
      }
      if constexpr (i == 0 && s == 3) {
#pragma unroll
        for (int m = 0; m < 5; ++m) tk[m] = lds_tr(lds + (m < 3 ? pk0 : pk1) + m * 512);
#pragma unroll
        for (int m = 0; m < 4; ++m) tv[m] = lds_tr(lds + pv0 + m * 512);
        shw = *(const LAS u32x2*)(lds + psh);
      }
      if constexpr (i == 2 && s == 3) {
        float kf[20], u[16];
#pragma unroll
        for (int m = 0; m < 5; ++m) { const u32x2 t = __builtin_bit_cast(u32x2, tk[m]);
          kf[4 * m] = bf_lo(t[0]); kf[4 * m + 1] = bf_hi(t[0]); kf[4 * m + 2] = bf_lo(t[1]); kf[4 * m + 3] = bf_hi(t[1]); }
        const float w0 = bf_lo(shw[0]), w1 = bf_hi(shw[0]), w2 = bf_lo(shw[1]), w3 = bf_hi(shw[1]);
#pragma unroll
        for (int m = 0; m < 4; ++m) { const u32x2 t = __builtin_bit_cast(u32x2, tv[m]);
          const float vv[4] = {bf_lo(t[0]), bf_hi(t[0]), bf_lo(t[1]), bf_hi(t[1])};
#pragma unroll
          for (int e = 0; e < 4; ++e) { const int n = 4 * m + e;
            u[n] = (kf[n + 1] * w3 + kf[n + 2] * w2 + kf[n + 3] * w1 + kf[n + 4] * w0) * vv[e]; } }
        u32x4 ua, ub_;
#pragma unroll
        for (int m = 0; m < 4; ++m) {
          const auto t = __builtin_amdgcn_permlane32_swap(pk2(u[15 - 2 * m], u[14 - 2 * m]), pk2(u[7 - 2 * m], u[6 - 2 * m]), false, false);
          ua[m] = t[0]; ub_[m] = t[1];
        }
        store16_dev(ua, rws, vu, WS_U);
        store16_dev(ub_, rws, vu, WS_U - 64);
      }
      if constexpr (i == 2 && (s == 5 || s == 6)) dma16i<(s - 5) * 1024, (s - 5) * 1024>(sHl, vl16, rh, sCh);
      if constexpr (i == 2 && (s == 7 || s == 8)) dma16i<H_ODD + (s - 7) * 1024, 2 + (s - 7) * 1024>(sHl, vl16, rh, sCh);
      if constexpr (i == 0 && s == 4) { *(LAS unsigned*)(lds + pzero) = 0; *(LAS unsigned*)(lds + L_SY + (l & 15) * 4) = 0; }
      if constexpr (i == 3 && s == 12) {
        *(LAS unsigned*)(lds + L_H4 + tg * H_PITCH + 32) = h01d; *(LAS unsigned*)(lds + L_H4 + H_ODD + tg * H_PITCH + 28) = h01d << 16;
      }
      if constexpr (i >= 2 && (s == 1 || s == 2)) {
        load8<32 * (2 * (s - 1) + i - 2)>(xv[4 * (s - 1) + 2 * (i - 2)], vxm, rx, cp * 128);
        load8<32 * (2 * (s - 1) + i - 2) + 16>(xv[4 * (s - 1) + 2 * (i - 2) + 1], vxm, rx, cp * 128);
      }
      if constexpr (i == 2 && s == 10)
        touch(warm_wo, l < 40 ? (384 + (tb2 & 1) * 320 + w * 40 + l) * 2048 : OOR, rwo, cp * 128);
      if constexpr (i == 0 && s == 13) PIN(warm_wo);
      if constexpr (i == 2 && s == 9) {
        float gs = 0.f;
#pragma unroll
        for (int m = 0; m < 4; ++m) gs += *(const LAS float*)(lds + L_GP + m * 256 + r32 * 4);
        __builtin_amdgcn_raw_buffer_store_b32(__builtin_bit_cast(unsigned, gs), rws, (w == 4 + hp && h == 0) ? r32 * 4 : OOR, WS_G + (tb * 128 + 32 * (hp & 3)) * 4, AUX_SC1);
      }
      if constexpr (i == 0 && s == 15) {
#pragma unroll
        for (int m = 0; m < 4; ++m) *(LAS f32x4*)(lds + L_OX + tg * 8192 + kh * 4096 + m * 1024 + vl16) = f32x4{o0[4 * m], o0[4 * m + 1], o0[4 * m + 2], o0[4 * m + 3]};
      }
      if constexpr (i == 0 && s == 11) flags_done<15>(fl, ws, WS_FU + hp * 128, tag, l);
      if constexpr (i >= 2 && s == 11) dma16i_dev<(i - 2) * 1024, WS_U + (i - 2) * 1024>(sUl, vl16, rws, sCh);
    };
#define EX(X, i) X[i] = __builtin_amdgcn_exp2f(X[i])
#define UNIT(PR, PR2, S, I, NEXT, HD, X, Y, OC, LC, OP, LP, P) do { \
    PRIO(PR); \
    if (HD && NEXT) { k0 = lds_b128(P); k1 = lds_b128((P) + 1024); } SBAR(); \
    EX(X, 0); EX(X, 1); SBAR(); \
    if constexpr ((S) >= 0 || (I) > 1) { OP = SMF32(v1, raw_hi(Y), OP); PIN(OP); } SBAR(); \
    if (!HD) v1 = lds_b128((P) + 3072); SBAR(); \
    EX(X, 2); EX(X, 3); SBAR(); \
    if constexpr ((S) >= 0 || (I) > 1) { LP = SMF16(ones, raw_hi(Y), LP); PIN(LP); } SBAR(); \
    side(Ic<S>{}, Ic<I>{}); SBAR(); \
    EX(X, 4); EX(X, 5); EX(X, 6); SBAR(); \
    if constexpr (NEXT) { Y = MFMA32(k0, qf[1 - HD][0], zero16); PIN(Y); } SBAR(); \
    if constexpr ((PR2) != (PR)) PRIO(PR2); \
    EX(X, 7); EX(X, 8); EX(X, 9); SBAR(); \
    if constexpr (NEXT) { Y = MFMA32(k1, qf[1 - HD][1], Y); PIN(Y); } SBAR(); \
    EX(X, 10); EX(X, 11); EX(X, 12); SBAR(); \
    OC = SMF32(v0, raw_lo(X), OC); PIN(OC); SBAR(); \
    if (HD && NEXT) v0 = lds_b128((P) + 2048); SBAR(); \
    EX(X, 13); EX(X, 14); EX(X, 15); PIN(X); SBAR(); \
    LC = SMF16(ones, raw_lo(X), LC); PIN(LC); SBAR(); } while (0)
    SFOR(s, 16)
      const lptr pc = kvp + (s % 3) * SLOT, pn = kvp + ((s + 1) % 3) * SLOT;
      UNIT(s < 15 ? 2 : 3, s < 15 ? 2 : 3, s - 1, 1, true, 0, sa, sb, o0, l0, o1, l1, pc);
      UNIT(2, 2, s - 1, 2, true, 1, sb, sa, o1, l1, o0, l0, pc + 4096);
      UNIT(s < 15 ? 1 : 2, s < 15 ? 0 : 2, s - 1, 3, true, 0, sa, sb, o0, l0, o1, l1, pc + 4096);
      constexpr int NW[15] = {7, 7, 8, 10, 8, 5, 5, 7, 6, 6, 6, 3, 5, 4, 0};
      if constexpr (s < 15) WAITBAR(NW[s]);
      UNIT(s < 15 ? 3 : 1, s < 15 ? 3 : 0, s, 0, s < 15, 1, sb, sa, o1, l1, o0, l0, pn);
    SEND
#undef UNIT
#undef EX
    *(LAS float*)(lds + L_LX + w * 256 + l * 4) = l0[0];
    BARRIER();
    f32x4 ox[4];
#pragma unroll
    for (int i = 0; i < 4; ++i) ox[i] = *(const LAS f32x4*)(lds + L_OX + tg * 8192 + (kh ^ 1) * 4096 + i * 1024 + vl16);
    const float lx = *(const LAS float*)(lds + L_LX + (w ^ 4) * 256 + l * 4);
    SBAR();
    o1 = SMF32(v1, raw_hi(sb), o1);
    l1 = SMF16(ones, raw_hi(sb), l1);
    SBAR();
    f32x16 oacc = o1;
#pragma unroll
    for (int i = 0; i < 16; ++i) oacc[i] += ox[i >> 2][i & 3];
    float lsum = lx + l1[0];
    const f32x8 s8 = __builtin_shufflevector(oacc, oacc, 0, 1, 2, 3, 4, 5, 6, 7) + __builtin_shufflevector(oacc, oacc, 8, 9, 10, 11, 12, 13, 14, 15);
    const f32x4 s4 = __builtin_shufflevector(s8, s8, 0, 1, 2, 3) + __builtin_shufflevector(s8, s8, 4, 5, 6, 7);
    const float asum = __builtin_fabsf((s4[0] + s4[2]) + (s4[1] + s4[3]));
    if (__builtin_expect(wave_any(!((lsum > 0x1p-60f) & (lsum < 0x1p100f) & (asum < __builtin_inff()))), 0)) {
      const bf16x8 q0 = qf[1][0], q1 = qf[1][1];
      float m = -__builtin_inff();
#pragma unroll 1
      for (int t = 0; t < 64; ++t) {
        const bf16x8 a = __builtin_bit_cast(bf16x8, __builtin_amdgcn_raw_buffer_load_b128(rws, vl16, WS_KV + t * 4096, AUX_SC1));
        const bf16x8 b = __builtin_bit_cast(bf16x8, __builtin_amdgcn_raw_buffer_load_b128(rws, vl16 + 1024, WS_KV + t * 4096, AUX_SC1));
        f32x16 sc = MFMA32(a, q0, zero16); sc = MFMA32(b, q1, sc);
#pragma unroll
        for (int i = 0; i < 16; ++i) m = __builtin_fmaxf(m, sc[i]);
      }
      m = __builtin_fmaxf(m, from_lane(m, l ^ 32));
      oacc = zero16; f32x4 la = {};
#pragma unroll 1
      for (int t = 0; t < 64; ++t) {
        const bf16x8 a = __builtin_bit_cast(bf16x8, __builtin_amdgcn_raw_buffer_load_b128(rws, vl16, WS_KV + t * 4096, AUX_SC1));
        const bf16x8 b = __builtin_bit_cast(bf16x8, __builtin_amdgcn_raw_buffer_load_b128(rws, vl16 + 1024, WS_KV + t * 4096, AUX_SC1));
        const bf16x8 c = __builtin_bit_cast(bf16x8, __builtin_amdgcn_raw_buffer_load_b128(rws, vl16 + 2048, WS_KV + t * 4096, AUX_SC1));
        const bf16x8 d = __builtin_bit_cast(bf16x8, __builtin_amdgcn_raw_buffer_load_b128(rws, vl16 + 3072, WS_KV + t * 4096, AUX_SC1));
        f32x16 sc = MFMA32(a, q0, zero16); sc = MFMA32(b, q1, sc);
#pragma unroll
        for (int i = 0; i < 16; ++i) sc[i] = __builtin_amdgcn_exp2f(sc[i] - m);
        oacc = SMF32(c, raw_lo(sc), oacc); la = SMF16(ones, raw_lo(sc), la);
        oacc = SMF32(d, raw_hi(sc), oacc); la = SMF16(ones, raw_hi(sc), la);
      }
      lsum = la[0];
    }
    const float rl = __builtin_amdgcn_rcpf(lsum); float ov[16];
#pragma unroll
    for (int i = 0; i < 16; ++i) ov[i] = oacc[i] * rl;
    const unsigned so = WS_O + (tb * 16 + hp) * 16384 + tg * 4096 + kh * 2048;
    store16_dev(__builtin_bit_cast(u32x4, pack8(ov)), rws, vl16, so);
    store16_dev(__builtin_bit_cast(u32x4, pack8(ov + 8)), rws, vl16, so + 1024);
  }
  asm volatile("s_barrier" ::: "memory");
  const unsigned fo_own = WS_FO + (((tb * 4 + tg) * 2 + kh) * 16 + hp) * 8;

  int conv_role = kh ^ 1;
  fresh(conv_role);
  if (conv_role == 0) {
    VMWAIT(0);
    put_flag(rws, vfw, fo_own, tag);
    PRIO(1);
    const unsigned swo = tg * 32768 + cp * 128, lwo = L0 + tg * 2048;
    dma16<7, 8192, 131072>(lwo + 8 * 8192, vW, rwo, swo + 8 * 131072);
    dma16<7, 8192, 131072>(lwo + 8 * 8192 + 1024, vW, rwo, swo + 8 * 131072 + 16384);
    wait_flags<31>(ws, WS_FO + (tb2 * 4 + tg) * 256, tag, l);
    VMWAIT(0);
    lds_signal(lds + L_SY + 16 + tg * 4);

    constexpr int OW = 32, SG = 46, YA = 41, YC = 47, YR = 53;
    u32x4 of[64]; unsigned gw;
    int kbo = cp * 13 / 16; kbo += kbo < 6 ? 0 : 2;
    int kbw = kbo;
    const unsigned so_in = WS_O + tb2 * 16 * 16384 + tg * 4096;
    auto next_blk = [](int& kb, int j) INL { if (j < 12) step_blk(kb); else kb = j == 12 ? 6 : j == 13 ? 7 : 15; };
    auto load_o = [&](auto i_c) INL {
      constexpr int i = decltype(i_c)::v;
      load16_dev<(i & 3) * 1024>(of[i], vl16, rws, so_in + kbo * 16384);
      if constexpr ((i & 3) == 3) next_blk(kbo, i >> 2);
    };
    load4_dev(gw, trow * 4, rws, WS_G);
    float gx[32], g, g1; u32x2 fl;
    auto issued = [](int i) { const int n = OW + 3 * i / 4; return n < 64 ? n : 64; };
    auto mul_g = [&](auto e_c) INL {
      constexpr int e = decltype(e_c)::v;
      if constexpr (e == 0) {
        PIN(gw); g = __builtin_amdgcn_rcpf(1.f + __builtin_amdgcn_exp2f(-1.4426950408889634f * __builtin_bit_cast(float, gw))); g1 = 1.f - g; PIN(g); PIN(g1);
      }
      PIN(xv[e]);
      gx[4 * e] = g * bf_lo(xv[e][0]); gx[4 * e + 1] = g * bf_hi(xv[e][0]); gx[4 * e + 2] = g * bf_lo(xv[e][1]); gx[4 * e + 3] = g * bf_hi(xv[e][1]);
      PIN(gx[4 * e]); PIN(gx[4 * e + 1]); PIN(gx[4 * e + 2]); PIN(gx[4 * e + 3]);
    };
    constexpr int G0 = OW - 8;
    SFOR(i, OW)
      load_o(Ic<i>{});
      if constexpr (i == G0 - 1) VMWAIT(G0);
      if constexpr (i >= G0) mul_g(Ic<i - G0>{});
    SEND
    const unsigned wa0 = L0 + (4 * h + q4) * 128 + (q4 >> 1) * 64 + 32 * (g4 & 1) + 8 * p4, wa1 = wa0 ^ 64;
    unsigned wb0 = 0, wb1 = 0, vy = (l >> 2) * 4096 + (l & 3) * 16, vyr = L0 + L_YT + tg * 4096 + (4 * h + q4) * 64 + 32 * (g4 & 1) + 8 * p4;
    PIN(vy); PIN(vyr);
    s16x4 wf[3][4];
    auto read_wo = [&](auto i_c, auto c_c) INL {
      constexpr int i = decltype(i_c)::v, c = decltype(c_c)::v;
      if constexpr ((i & 3) == 0 && c == 0) { wb0 = wa0 + kbw * 8192; wb1 = wa1 + kbw * 8192; next_blk(kbw, i >> 2); }
      read_tr2<(i & 3) * 2048>(wf[i % 3][2 * c], wf[i % 3][2 * c + 1], c ? wb1 : wb0);
    };
    auto frag = [](const s16x4& a, const s16x4& b) INL { return __builtin_bit_cast(bf16x8, __builtin_shufflevector(a, b, 0, 1, 2, 3, 4, 5, 6, 7)); };
    lds_await(lds + L_SY + 16, ws);
    read_wo(Ic<0>{}, Ic<0>{}); read_wo(Ic<0>{}, Ic<1>{}); read_wo(Ic<1>{}, Ic<0>{}); read_wo(Ic<1>{}, Ic<1>{});
    f32x16 acc0 = {}, acc1 = {}; s16x4 yt[8];
    auto mul_y = [&](int n) INL {
      if (n < 16) {
        const unsigned t = __builtin_bit_cast(u32x2, yt[n >> 1])[n & 1];
        gx[2 * n] *= bf_lo(t); gx[2 * n + 1] *= bf_hi(t);
        PIN(gx[2 * n]); PIN(gx[2 * n + 1]);
      }
    };
    SFOR(i, 62)
      if constexpr ((i & 3) == 0) {
        static_assert(issued(i) >= i + 8 || issued(i) == 64, "loads run ahead of their steps");
        if constexpr (i <= YC) vmwait_for<issued(i) - i - 4>(of[i]); else PIN(of[i]);
        PIN(of[i + 1]); PIN(of[i + 2]); PIN(of[i + 3]);
      }
      lgkmwait_for<4>(wf[i % 3][0], wf[i % 3][1], wf[i % 3][2], wf[i % 3][3]);
      if constexpr (i >= YR + 2 && i < YR + 4) { PIN(yt[4 * (i - YR - 2)]); PIN(yt[4 * (i - YR - 2) + 1]); PIN(yt[4 * (i - YR - 2) + 2]); PIN(yt[4 * (i - YR - 2) + 3]); }
      SBAR();
      acc0 = MFMA32(frag(wf[i % 3][0], wf[i % 3][1]), __builtin_bit_cast(bf16x8, of[i]), acc0); PIN(acc0);
      SBAR();
      if constexpr (i == YA) flags_ask<63>(fl, rws, WS_FY + cp * 512, l);
      if constexpr (i == YC) {
        static_assert(issued(i) == 64 && YC > SG, "all O loads are requested, the conv operands' LDS is free");
        VMWAIT(0); flags_done<63>(fl, ws, WS_FY + cp * 512, tag, l);
        dma16_dev<4, 1024, 65536>(L0 + L_YT + tg * 4096, vy, rws, WS_Y + cp * 64 * 4096 + (tb2 * 128 + 32 * tg) * 2);
      }
      if constexpr (i == YR) VMWAIT(0);
      if constexpr (i >= YR && i < YR + 2) {
        read_tr2y<(i - YR) * 2048>(yt[4 * (i - YR)], yt[4 * (i - YR) + 1], vyr); read_tr2y<(i - YR) * 2048 + 1024>(yt[4 * (i - YR) + 2], yt[4 * (i - YR) + 3], vyr);
      }
      if constexpr (i + 2 < 64) read_wo(Ic<i + 2>{}, Ic<0>{});
      SFOR(j, issued(i + 1) - issued(i)) load_o(Ic<issued(i) + j>{}); SEND
      if constexpr (i >= YR + 2) mul_y(2 * (i - YR - 2));
      SBAR();
      acc1 = MFMA32(frag(wf[i % 3][2], wf[i % 3][3]), __builtin_bit_cast(bf16x8, of[i]), acc1); PIN(acc1);
      SBAR();
      if constexpr (i + 2 < 64) read_wo(Ic<i + 2>{}, Ic<1>{});
      if constexpr (i >= YR + 2) mul_y(2 * (i - YR - 2) + 1);
      if constexpr (i == SG) lds_await(lds + L_SY + 32, ws);
      SBAR();
    SEND
    static_assert(YR + 4 <= 62, "the y tile is in registers");
    const rsrc_t ro = mk_rsrc(pout, 4u << 20);
    auto put_out = [&](int c, const f32x16& b) INL {
      u32x2 ou[4];
#pragma unroll
      for (int i = 0; i < 4; ++i) {
        const float* t = gx + 16 * c + 4 * i;
        ou[i] = u32x2{pk2(t[0] + g1 * b[4 * i], t[1] + g1 * b[4 * i + 1]), pk2(t[2] + g1 * b[4 * i + 2], t[3] + g1 * b[4 * i + 3])};
      }
#pragma unroll
      for (int i = 0; i < 4; i += 2) {
        const auto a = __builtin_amdgcn_permlane32_swap(ou[i][0], ou[i + 1][0], false, false);
        const auto d = __builtin_amdgcn_permlane32_swap(ou[i][1], ou[i + 1][1], false, false);
        const u32x4 o = {a[0], d[0], a[1], d[1]};
        __builtin_amdgcn_raw_buffer_store_b128(o, ro, trow * 2048 + (64 * cp + 32 * c + 8 * i + 8 * h) * 2, 0, 0);
      }
    };
    lgkmwait_for<0>(wf[62 % 3][0], wf[62 % 3][1], wf[62 % 3][2], wf[62 % 3][3]);
    lgkmwait_for<0>(wf[63 % 3][0], wf[63 % 3][1], wf[63 % 3][2], wf[63 % 3][3]);
    SBAR();
    SFOR(j, 2) acc0 = MFMA32(frag(wf[(62 + j) % 3][0], wf[(62 + j) % 3][1]), __builtin_bit_cast(bf16x8, of[62 + j]), acc0); PIN(acc0); SEND
    SFOR(j, 2) acc1 = MFMA32(frag(wf[(62 + j) % 3][2], wf[(62 + j) % 3][3]), __builtin_bit_cast(bf16x8, of[62 + j]), acc1); PIN(acc1); SEND
    SBAR();
#pragma unroll
    for (int n = 2 * (60 - YR); n < 16; ++n) mul_y(n);
    put_out(0, acc0);
    SBAR();
    put_out(1, acc1);
    put_flag(rws, bx == 0 ? vflag : OOR, WS_EPOCH, tag);
    asm volatile("fused_code_end_b:");
  } else {
    SFOR(i, 8) READ_H(i, 0); READ_U(7 - i, 0); SEND
    PRIO(2);
    f32x4 acc[8];
#define CONV_MF(a, be, de) conv_mfma<rr == 0 && (be) == 0>(acc[a], hl[rr & 1][be], hh[rr & 1][be], ub[de])
    SFOR(rr, 8)
      SFOR(dn, 8)
        constexpr int de = 7 - dn;
        if constexpr (rr == 0 && de == 7) asm volatile("s_waitcnt lgkmcnt(15)" ::: "memory");
        else if constexpr ((rr == 0 && de == 5) || (rr == 7 && de == 7)) asm volatile("s_waitcnt lgkmcnt(0)" ::: "memory");
        else if constexpr (rr < 7 && (de == 7 || de == 3)) asm volatile("s_waitcnt lgkmcnt(4)" ::: "memory");
        SFOR(be, 8 - de)
          CONV_MF(be + de, be, de);
          constexpr int n = dn * (dn + 1) / 2 + be;
          if constexpr (rr < 7 && n >= 6 && n < 30 && n % 3 == 0) READ_H((n - 6) / 3, rr + 1);
          if constexpr (rr < 7 && n >= 10 && n < 31 && n % 3 == 1) READ_U(7 - (n - 10) / 3, rr + 1);
          if constexpr (rr < 7 && n == 35) READ_U(0, rr + 1);
        SEND
        if constexpr (rr == 0 && de == 0) { VMWAIT(0); put_flag(rws, vfw, fo_own, tag); }
        if constexpr (rr == 7 && de == 7) lds_signal(lds + L_SY + tg * 4);
      SEND
    SEND
    asm volatile("s_nop 7" : "+v"(acc[0]), "+v"(acc[1]), "+v"(acc[2]), "+v"(acc[3]), "+v"(acc[4]), "+v"(acc[5]), "+v"(acc[6]), "+v"(acc[7]));
#pragma unroll
    for (int a = 0; a < 8; a += 2) {
      const auto p = __builtin_amdgcn_permlane16_swap(pk2(acc[a][0], acc[a][1]), pk2(acc[a + 1][0], acc[a + 1][1]), false, false);
      const auto q = __builtin_amdgcn_permlane16_swap(pk2(acc[a][2], acc[a][3]), pk2(acc[a + 1][2], acc[a + 1][3]), false, false);
      store16_dev(u32x4{p[0], q[0], p[1], q[1]}, rws, (256 * (g4 & 1) + 16 * i16 + 8 * (g4 >> 1)) * 2 + 512 * a, WS_Y + sCh);
    }
    VMWAIT(0);
    put_flag(rws, vfw, WS_FY + ((hp * 16 + tb) * 4 + tg) * 8, tag);
    PRIO(0);
    lds_await(lds + L_SY, ws);
    const unsigned swo = tg * 32768 + cp * 128, lwo = L0 + tg * 2048;
    dma16<2, 8192, 131072>(lwo + 6 * 8192, vW, rwo, swo + 6 * 131072);
    dma16<2, 8192, 131072>(lwo + 6 * 8192 + 1024, vW, rwo, swo + 6 * 131072 + 16384);
    dma16<2, 1024, 16384>(lwo + 15 * 8192, vW, rwo, swo + 15 * 131072);
    VMWAIT(0);
    lds_signal(lds + L_SY + 32 + tg * 4);
    asm volatile("fused_code_end_a:" ::: "memory");
  }
}

extern "C" void kernel_launch(void* const* d_in, const int* in_sizes, int n_in, void* d_out, int out_size, void* d_ws, size_t ws_size,
                              hipStream_t stream) {
  typedef const __hip_bfloat16* P;
  fused_kernel<<<256, 512, 0, stream>>>((P)d_in[0], (P)d_in[1], (P)d_in[2], (P)d_in[3], (P)d_in[4], (P)d_in[5], (P)d_in[7], (P)d_in[8],
                                        (P)d_in[9], (P)d_in[10], (P)d_in[11], (__hip_bfloat16*)d_out, (char*)d_ws);
}
```
